```python
import torch

def _softmax(x, axis=-1):
    return torch.softmax(x, dim=axis)
def _gelu(x):
    return torch.nn.functional.gelu(x, approximate="tanh")
def _silu(x):
    return torch.nn.functional.silu(x)
def _sigmoid(x):
    return torch.sigmoid(x)
def _layernorm(x, axis=-1, eps=1e-5):
    m = x.mean(dim=axis, keepdim=True)
    v = x.var(dim=axis, keepdim=True, unbiased=False)
    return (x - m) / torch.sqrt(v + eps)
def _scatter_add(tgt, idx, src, axis=0):
    return tgt.clone().index_add_(axis, idx, src)
def _segment_sum(src, seg, n):
    out = torch.zeros((n,) + src.shape[1:], dtype=src.dtype)
    return out.index_add_(0, seg, src)
def _pad(x, pw):
    flat = tuple(v for pair in reversed(pw) for v in pair)
    return torch.nn.functional.pad(x, flat)

kernel_name = 'concat_heads__sliding_local_dilated__grouped_kv_mqa__prompt'

def setup_inputs():

    _w = (lambda a: (lambda t: t.to(torch.bfloat16) if t.is_floating_point() else t)(torch.as_tensor(a)))
    def _A_setup():
        B, S, D = 1, 2048, 512
        return {
            "x": _w((lambda *s: torch.randn(*s, dtype=torch.bfloat16) * 0.1)(B, S, D)),
            "wqkv": _w((lambda *s: torch.randn(*s, dtype=torch.bfloat16) * 0.8)(D, 3 * D)),
            "wo": _w((lambda *s: torch.randn(*s, dtype=torch.bfloat16) * 0.8)(D, D)),
        }
    _A_in = _A_setup()

    def _B_setup():
        B, S, D, H = 1, 2048, 512, 32
        Dh = D // H
        return {
            "x": _w((lambda *s: torch.randn(*s, dtype=torch.bfloat16) * 0.05)(B, S, D)),
            "wq": _w((lambda *s: torch.randn(*s, dtype=torch.bfloat16) * 3.2)(D, H * Dh)),
            "wk": _w((lambda *s: torch.randn(*s, dtype=torch.bfloat16) * 3.2)(D, Dh)),
            "wv": _w((lambda *s: torch.randn(*s, dtype=torch.bfloat16) * 1.6)(D, Dh)),
            "wo": _w((lambda *s: torch.randn(*s, dtype=torch.bfloat16) * 1.6)(H * Dh, D)),
        }
    _B_in = _B_setup()

    return {
        "A_x": _A_in["x"],
        "A_wqkv": _A_in["wqkv"],
        "A_wo": _A_in["wo"],
        "B_x": _B_in["x"],
        "B_wq": _B_in["wq"],
        "B_wk": _B_in["wk"],
        "B_wv": _B_in["wv"],
        "B_wo": _B_in["wo"],
    }

def reference(A_x, A_wqkv, A_wo, B_x, B_wq, B_wk, B_wv, B_wo):
    def _A_ref(x, wqkv, wo):
        B, S, D = x.shape
        H = 32; Dh = D // H
        W, dil = 8, 2
        qkv = (lambda x, *d: x.permute(*d))((x @ wqkv).reshape(B, S, 3, H, Dh), 2, 0, 3, 1, 4)
        q, k, v = qkv[0], qkv[1], qkv[2]
        scale = Dh ** -0.5
        pos = torch.arange(S)
        off = torch.arange(W) * dil
        idx = pos[:, None] - off[None, :]
        mask = idx >= 0
        idx_c = torch.where(mask, idx, 0).reshape(-1)
        kg = k[:, :, idx_c, :].reshape(B, H, S, W, Dh)
        vg = v[:, :, idx_c, :].reshape(B, H, S, W, Dh)
        logits = torch.einsum("bhsd,bhswd->bhsw", q, kg) * scale
        logits = torch.where(mask[None, None, :, :], logits, -1e9)
        attn = _softmax(logits, axis=-1)
        o = torch.einsum("bhsw,bhswd->bhsd", attn, vg)
        return (lambda x, *d: x.permute(*d))(o, 0, 2, 1, 3).reshape(B, S, D) @ wo

    def _B_ref(x, wq, wk, wv, wo):
        B, S, D = x.shape
        H = 32
        Dh = D // H
        q = (lambda x, *d: x.permute(*d))((x @ wq).reshape(B, S, H, Dh), 0, 2, 1, 3)
        k = x @ wk
        v = x @ wv
        scores = torch.einsum("bhqd,bkd->bhqk", q, k) * (Dh ** -0.5)
        p = _softmax(scores, axis=-1)
        o = torch.einsum("bhqk,bkd->bhqd", p, v)
        return (lambda x, *d: x.permute(*d))(o, 0, 2, 1, 3).reshape(B, S, H * Dh) @ wo

    a = _A_ref(A_x, A_wqkv, A_wo)
    b = _B_ref(B_x, B_wq, B_wk, B_wv, B_wo)
    return torch.concatenate([a, b], axis=-1)
```

```cpp
#include <hip/hip_runtime.h>
#include <hip/hip_bf16.h>
#include <stdint.h>

typedef short bf16x8 __attribute__((ext_vector_type(8)));
typedef short s16x4 __attribute__((ext_vector_type(4)));
typedef short v4i16_t __attribute__((ext_vector_type(4)));
typedef float f32x16 __attribute__((ext_vector_type(16)));
typedef float f32x4 __attribute__((ext_vector_type(4)));
typedef float f32x2 __attribute__((ext_vector_type(2)));
typedef __bf16 bf16x2_t __attribute__((ext_vector_type(2)));
typedef unsigned u32x4 __attribute__((ext_vector_type(4)));
typedef unsigned u32x2 __attribute__((ext_vector_type(2)));
#define LAS __attribute__((address_space(3)))
#define DI __device__ __forceinline__
#define MFMA32(a, b, c) __builtin_amdgcn_mfma_f32_32x32x16_bf16((a), (b), (c), 0, 0, 0)

constexpr int SEQ = 2048, DM = 512;
constexpr float QSCALE = 0.25f * 1.4426950408889634f;

constexpr size_t WS_QF = 0;
constexpr size_t WS_OA = 2u << 20;
constexpr size_t WS_OB = 4u << 20;
constexpr size_t WS_KF = 6u << 20;
constexpr size_t WS_VF = WS_KF + 65536;

DI unsigned pk_bf16(float lo, float hi) {
  f32x2 v = {lo, hi};
  bf16x2_t r = __builtin_convertvector(v, bf16x2_t);
  return __builtin_bit_cast(unsigned, r);
}
DI float bf_lo(unsigned u) { return __uint_as_float(u << 16); }
DI float bf_hi(unsigned u) { return __uint_as_float(u & 0xffff0000u); }
DI void glds16(const void* g, LAS char* l) { __builtin_amdgcn_global_load_lds((const unsigned*)g, (LAS unsigned*)l, 16, 0, 0); }
DI bf16x8 lds_rd128(LAS const char* p) { return *(LAS const bf16x8*)p; }
DI s16x4 lds_tr(LAS const char* p) { return __builtin_bit_cast(s16x4, __builtin_amdgcn_ds_read_tr16_b64_v4i16((LAS v4i16_t*)p)); }
DI bf16x8 cat8(s16x4 a, s16x4 b) { return __builtin_shufflevector(a, b, 0, 1, 2, 3, 4, 5, 6, 7); }
#define WAIT_VM0() asm volatile("s_waitcnt vmcnt(0)" ::: "memory")

DI void stage_x(const uint16_t* x, int row0, int nrows, LAS char* dst, int wid, int lane, int nw) {
  for (int r = wid; r < nrows; r += nw) {
    int gr = row0 + r; gr = gr < 0 ? 0 : gr;
    glds16(x + (size_t)gr * DM + ((lane ^ (r & 15)) << 3), dst + r * 1024);
  }
}
DI void stage_w64(const uint16_t* w, int ldw, int col0, int k0, int nk, LAS char* dst, int wid, int lane, int nw) {
  for (int m = wid; m < (nk >> 3); m += nw) {
    int kl = m * 8 + (lane >> 3);
    int chunk = (lane & 7) ^ (((kl >> 1) & 1) << 2);
    glds16(w + (size_t)(k0 + kl) * ldw + col0 + (chunk << 3), dst + m * 1024);
  }
}
DI int wtr_base(int lane, int n0) {
  int g = lane >> 4, cb = g & 1, i = lane & 15, q = i >> 2, p = i & 3;
  int cidx = (n0 >> 3) + 2 * cb + (p >> 1);
  return q * 128 + ((cidx ^ ((q >> 1) << 2)) << 4) + (p & 1) * 8;
}
DI bf16x8 xfrag(LAS const char* xb, int pt, int s, int lane) {
  int r = lane & 31, h = lane >> 5;
  int c = 2 * s + h;
  return lds_rd128(xb + (pt * 32 + r) * 1024 + ((c ^ (r & 15)) << 4));
}

constexpr int LDS_BYTES = 160 * 1024;

__global__ __launch_bounds__(512, 2) void k_proj(const uint16_t* __restrict__ ax, const uint16_t* __restrict__ awqkv,
                                                const uint16_t* __restrict__ bx, const uint16_t* __restrict__ bwq,
                                                const uint16_t* __restrict__ bwk, const uint16_t* __restrict__ bwv, char* __restrict__ ws) {
  __shared__ __attribute__((aligned(16))) char smem_[LDS_BYTES];
  LAS char* smem = (LAS char*)smem_;
  const int tid = threadIdx.x, lane = tid & 63, wid = __builtin_amdgcn_readfirstlane(tid >> 6);
  const int b = blockIdx.x, xcd = b & 7, idx = b >> 3, pb = xcd * 4 + (idx >> 3), hg = idx & 7;
  const int r = lane & 31, h = lane >> 5;

  {
    LAS char* XB = smem;
    LAS char* WQ = smem + 65536;
    LAS char* WKV = smem + 131072;
    const bool kvprod = hg < 2;
    if (kvprod) {
      for (int m = wid; m < 32; m += 8) {
        int k = m * 16 + (lane >> 2), slot = lane & 3;
        const uint16_t* src = (slot < 2 ? bwk : bwv) + k * 16 + (slot & 1) * 8;
        glds16(src, WKV + m * 1024);
      }
    }
    stage_x(bx, pb * 64, 64, XB, wid, lane, 8);
    stage_w64(bwq, 512, hg * 64, 0, 512, WQ, wid, lane, 8);
    WAIT_VM0();
    __syncthreads();
    f32x16 kd1, kd2;
    if (kvprod) {
      for (int i = 0; i < 16; ++i) { kd1[i] = 0.f; kd2[i] = 0.f; }
      const int g = lane >> 4, cb = g & 1, hh = g >> 1, i16 = lane & 15, q = i16 >> 2, p = i16 & 3;
      LAS const char* wb = WKV + (8 * hh + q) * 64 + cb * 32 + p * 8;
      #pragma unroll
      for (int ss = 0; ss < 4; ++ss) {
        int s = wid * 4 + ss;
        bf16x8 wf = cat8(lds_tr(wb + s * 1024), lds_tr(wb + s * 1024 + 256));
        bf16x8 xf = xfrag(XB, hg, s, lane);
        kd1 = MFMA32(wf, xf, kd1);
        kd2 = MFMA32(xf, wf, kd2);
      }
    }
    const int ch = wid & 1, ph = (wid >> 1) & 1, ks = wid >> 2;
    f32x16 acc;
    for (int i = 0; i < 16; ++i) acc[i] = 0.f;
    {
      LAS const char* wb = WQ + wtr_base(lane, ch * 32) + (8 * h) * 128;
      #pragma unroll
      for (int ss = 0; ss < 16; ++ss) {
        int s = ks * 16 + ss;
        bf16x8 wf = cat8(lds_tr(wb + s * 2048), lds_tr(wb + s * 2048 + 512));
        bf16x8 xf = xfrag(XB, ph, s, lane);
        acc = MFMA32(wf, xf, acc);
      }
    }
    __syncthreads();
    LAS float* red = (LAS float*)smem;
    LAS float* kvr = (LAS float*)(smem + 16384);
    if (ks == 1) {
      #pragma unroll
      for (int i = 0; i < 16; ++i) red[((wid & 3) * 16 + i) * 64 + lane] = acc[i];
    }
    if (kvprod) {
      LAS float* my = kvr + wid * 1024;
      *(LAS f32x4*)(my + lane * 8) = f32x4{kd1[0], kd1[1], kd1[2], kd1[3]};
      *(LAS f32x4*)(my + lane * 8 + 4) = f32x4{kd1[4], kd1[5], kd1[6], kd1[7]};
      if (r >= 16) {
        int d = r - 16;
        #pragma unroll
        for (int s = 0; s < 2; ++s) {
          LAS float* pv = my + 512 + (((s * 2 + h) * 16 + d) * 8);
          *(LAS f32x4*)(pv) = f32x4{kd2[8 * s + 0], kd2[8 * s + 1], kd2[8 * s + 2], kd2[8 * s + 3]};
          *(LAS f32x4*)(pv + 4) = f32x4{kd2[8 * s + 4], kd2[8 * s + 5], kd2[8 * s + 6], kd2[8 * s + 7]};
        }
      }
    }
    __syncthreads();
    if (ks == 0) {
      #pragma unroll
      for (int i = 0; i < 16; ++i) acc[i] = (acc[i] + red[((wid & 3) * 16 + i) * 64 + lane]) * QSCALE;
      #pragma unroll
      for (int a = 0; a < 2; ++a) {
        u32x4 o = {pk_bf16(acc[8 * a + 0], acc[8 * a + 1]), pk_bf16(acc[8 * a + 2], acc[8 * a + 3]),
                   pk_bf16(acc[8 * a + 4], acc[8 * a + 5]), pk_bf16(acc[8 * a + 6], acc[8 * a + 7])};
        int head = hg * 4 + ch * 2 + a, pt = pb * 2 + ph;
        *(u32x4*)(ws + WS_QF + ((size_t)(pt * 32 + head) * 64 + lane) * 16) = o;
      }
    }
    if (kvprod) {
      float s0 = 0.f, s1 = 0.f;
      #pragma unroll
      for (int w = 0; w < 8; ++w) { f32x2 v = *(LAS f32x2*)(kvr + w * 1024 + tid * 2); s0 += v[0]; s1 += v[1]; }
      unsigned o = pk_bf16(s0, s1);
      int kt = pb * 2 + hg;
      if (tid < 256) *(unsigned*)(ws + WS_KF + kt * 1024 + tid * 4) = o;
      else *(unsigned*)(ws + WS_VF + kt * 1024 + (tid - 256) * 4) = o;
    }
    __syncthreads();
  }

  {
    LAS char* XA = smem;
    LAS char* WR = smem + 96 * 1024;
    stage_x(ax, pb * 64 - 32, 96, XA, wid, lane, 8);
    auto stage_chunk = [&](int c, int buf) {
      LAS char* d = WR + buf * 24576;
      #pragma unroll
      for (int sl = 0; sl < 3; ++sl) {
        int kl = wid * 8 + (lane >> 3);
        int chunk = (lane & 7) ^ (((kl >> 1) & 1) << 2);
        glds16(awqkv + (size_t)(c * 64 + kl) * 1536 + sl * 512 + hg * 64 + (chunk << 3), d + sl * 8192 + wid * 1024);
      }
    };
    stage_chunk(0, 0);
    const int ch = wid & 1, grp = wid >> 1;
    const int sl0 = grp < 3 ? grp : 1, sl1 = grp < 3 ? grp : 2;
    const int pt0 = grp < 3 ? 1 : 0, pt1 = grp < 3 ? 2 : 0;
    f32x16 acc0, acc1;
    for (int i = 0; i < 16; ++i) { acc0[i] = 0.f; acc1[i] = 0.f; }
    const int wtb = wtr_base(lane, ch * 32) + (8 * h) * 128;
    for (int c = 0; c < 8; ++c) {
      WAIT_VM0();
      __syncthreads();
      if (c + 1 < 8) stage_chunk(c + 1, (c + 1) & 1);
      LAS const char* d = WR + (c & 1) * 24576;
      #pragma unroll
      for (int ss = 0; ss < 4; ++ss) {
        int s = c * 4 + ss;
        bf16x8 w0 = cat8(lds_tr(d + sl0 * 8192 + wtb + ss * 2048), lds_tr(d + sl0 * 8192 + wtb + ss * 2048 + 512));
        bf16x8 w1 = cat8(lds_tr(d + sl1 * 8192 + wtb + ss * 2048), lds_tr(d + sl1 * 8192 + wtb + ss * 2048 + 512));
        bf16x8 x0 = xfrag(XA, pt0, s, lane);
        bf16x8 x1 = xfrag(XA, pt1, s, lane);
        acc0 = MFMA32(w0, x0, acc0);
        acc1 = MFMA32(w1, x1, acc1);
      }
    }
    __syncthreads();
    LAS char* QKV = smem;
    #pragma unroll
    for (int a = 0; a < 2; ++a) {
      int head = ch * 2 + a;
      u32x4 o0 = {pk_bf16(acc0[8 * a + 0], acc0[8 * a + 1]), pk_bf16(acc0[8 * a + 2], acc0[8 * a + 3]),
                  pk_bf16(acc0[8 * a + 4], acc0[8 * a + 5]), pk_bf16(acc0[8 * a + 6], acc0[8 * a + 7])};
      u32x4 o1 = {pk_bf16(acc1[8 * a + 0], acc1[8 * a + 1]), pk_bf16(acc1[8 * a + 2], acc1[8 * a + 3]),
                  pk_bf16(acc1[8 * a + 4], acc1[8 * a + 5]), pk_bf16(acc1[8 * a + 6], acc1[8 * a + 7])};
      *(LAS u32x4*)(QKV + ((((sl0 * 4 + head) * 2 + h) * 96) + pt0 * 32 + r) * 16) = o0;
      *(LAS u32x4*)(QKV + ((((sl1 * 4 + head) * 2 + h) * 96) + pt1 * 32 + r) * 16) = o1;
    }
    __syncthreads();
    {
      const int pt = 1 + (wid & 1), head = wid >> 1;
      const int pl = pt * 32 + r;
      const int gpos = pb * 64 - 32 + pl;
      LAS const char* qb = QKV + (((0 * 4 + head) * 2 + h) * 96) * 16;
      LAS const char* kb = QKV + (((1 * 4 + head) * 2 + h) * 96) * 16;
      LAS const char* vb = QKV + (((2 * 4 + head) * 2 + h) * 96) * 16;
      u32x4 q = *(LAS const u32x4*)(qb + pl * 16);
      float qf[8];
      #pragma unroll
      for (int i = 0; i < 4; ++i) { qf[2 * i] = bf_lo(q[i]); qf[2 * i + 1] = bf_hi(q[i]); }
      float lg[8];
      #pragma unroll
      for (int w = 0; w < 8; ++w) {
        u32x4 kk = *(LAS const u32x4*)(kb + (pl - 2 * w) * 16);
        float s = 0.f;
        #pragma unroll
        for (int i = 0; i < 4; ++i) { s = fmaf(qf[2 * i], bf_lo(kk[i]), s); s = fmaf(qf[2 * i + 1], bf_hi(kk[i]), s); }
        s += __shfl_xor(s, 32);
        lg[w] = (gpos - 2 * w >= 0) ? s * QSCALE : -INFINITY;
      }
      float m = lg[0];
      #pragma unroll
      for (int w = 1; w < 8; ++w) m = fmaxf(m, lg[w]);
      float l = 0.f;
      #pragma unroll
      for (int w = 0; w < 8; ++w) { lg[w] = __builtin_amdgcn_exp2f(lg[w] - m); l += lg[w]; }
      float inv = 1.0f / l;
      float o[8];
      #pragma unroll
      for (int j = 0; j < 8; ++j) o[j] = 0.f;
      #pragma unroll
      for (int w = 0; w < 8; ++w) {
        u32x4 vv = *(LAS const u32x4*)(vb + (pl - 2 * w) * 16);
        float p = lg[w] * inv;
        #pragma unroll
        for (int i = 0; i < 4; ++i) { o[2 * i] = fmaf(p, bf_lo(vv[i]), o[2 * i]); o[2 * i + 1] = fmaf(p, bf_hi(vv[i]), o[2 * i + 1]); }
      }
      u32x4 ov = {pk_bf16(o[0], o[1]), pk_bf16(o[2], o[3]), pk_bf16(o[4], o[5]), pk_bf16(o[6], o[7])};
      int ptile = pb * 2 + (wid & 1), ghead = hg * 4 + head;
      *(u32x4*)(ws + WS_OA + ((size_t)(ptile * 32 + ghead) * 64 + lane) * 16) = ov;
    }
  }
}

constexpr float M_OFF = 40.f;
constexpr int VC_OFF = 131072;

DI void attn_pass(LAS const char* KL, LAS const char* vaddr, int vstride, const bf16x8& qa, const bf16x8& qb,
                  const f32x16& ca, const f32x16& cb, f32x16& oa, f32x16& ob) {
  for (int t4 = 0; t4 < 8; ++t4) {
    #pragma unroll
    for (int tt = 0; tt < 4; ++tt) {
      bf16x8 kf = lds_rd128(KL + tt * 1024);
      bf16x8 v0 = lds_rd128(vaddr + tt * 1024);
      bf16x8 v1 = lds_rd128(vaddr + tt * 1024 + 512);
      f32x16 sa = MFMA32(kf, qa, ca);
      f32x16 sb = MFMA32(kf, qb, cb);
      u32x4 pa0, pa1, pb0, pb1;
      #pragma unroll
      for (int i = 0; i < 4; ++i) {
        pa0[i] = pk_bf16(__builtin_amdgcn_exp2f(sa[2 * i]), __builtin_amdgcn_exp2f(sa[2 * i + 1]));
        pa1[i] = pk_bf16(__builtin_amdgcn_exp2f(sa[8 + 2 * i]), __builtin_amdgcn_exp2f(sa[8 + 2 * i + 1]));
      }
      oa = MFMA32(v0, __builtin_bit_cast(bf16x8, pa0), oa);
      oa = MFMA32(v1, __builtin_bit_cast(bf16x8, pa1), oa);
      #pragma unroll
      for (int i = 0; i < 4; ++i) {
        pb0[i] = pk_bf16(__builtin_amdgcn_exp2f(sb[2 * i]), __builtin_amdgcn_exp2f(sb[2 * i + 1]));
        pb1[i] = pk_bf16(__builtin_amdgcn_exp2f(sb[8 + 2 * i]), __builtin_amdgcn_exp2f(sb[8 + 2 * i + 1]));
      }
      ob = MFMA32(v0, __builtin_bit_cast(bf16x8, pb0), ob);
      ob = MFMA32(v1, __builtin_bit_cast(bf16x8, pb1), ob);
    }
    KL += 4096;
    vaddr += vstride;
  }
}
DI float max16(const f32x16& s) {
  float m0 = fmaxf(fmaxf(s[0], s[1]), s[2]), m1 = fmaxf(fmaxf(s[3], s[4]), s[5]);
  float m2 = fmaxf(fmaxf(s[6], s[7]), s[8]), m3 = fmaxf(fmaxf(s[9], s[10]), s[11]);
  float m4 = fmaxf(fmaxf(s[12], s[13]), s[14]);
  return fmaxf(fmaxf(fmaxf(m0, m1), fmaxf(m2, m3)), fmaxf(m4, s[15]));
}

__global__ __launch_bounds__(512, 2) void k_attn(char* __restrict__ ws) {
  __shared__ __attribute__((aligned(16))) char smem_[131072 + 4096];
  LAS char* smem = (LAS char*)smem_;
  const int tid = threadIdx.x, lane = tid & 63, wid = __builtin_amdgcn_readfirstlane(tid >> 6);
  const int b = blockIdx.x, xcd = b & 7, idx = b >> 3, pb = xcd * 4 + (idx >> 3), hg = idx & 7;
  const int r = lane & 31, h = lane >> 5;
  const int ch = wid & 1, ph = (wid >> 1) & 1, kh = wid >> 2;
  for (int m = wid; m < 128; m += 8) glds16(ws + WS_KF + m * 1024 + lane * 16, smem + m * 1024);
  if (tid < 8 * 8) {
    int blk = tid >> 3, sl = tid & 7;
    unsigned v = ((sl & 3) == 0) ? 0x3f803f80u : 0u;
    *(LAS u32x4*)(smem + VC_OFF + blk * 512 + sl * 16) = u32x4{v, v, v, v};
  }
  const int pt = pb * 2 + ph, head0 = hg * 4 + ch * 2;
  bf16x8 qa = *(const bf16x8*)(ws + WS_QF + ((size_t)(pt * 32 + head0) * 64 + lane) * 16);
  bf16x8 qb = *(const bf16x8*)(ws + WS_QF + ((size_t)(pt * 32 + head0 + 1) * 64 + lane) * 16);
  WAIT_VM0();
  __syncthreads();

  LAS const char* KL = smem + kh * 32768 + lane * 16;
  LAS const char* vaddr; int vstride;
  if (r < 16) { vaddr = smem + 65536 + kh * 32768 + h * 256 + r * 16; vstride = 4096; }
  else {
    bool g1 = (r >= 20 && r < 28);
    bool ones = (r == 16 || r == 20);
    vaddr = smem + VC_OFF + (g1 ? 64 : 0) + (ones ? 0 : 16); vstride = 0;
  }
  f32x16 zero; for (int i = 0; i < 16; ++i) zero[i] = 0.f;
  float ma, mb;
  {
    bf16x8 k0 = lds_rd128(KL), k1 = lds_rd128(KL + 1024);
    ma = fmaxf(max16(MFMA32(k0, qa, zero)), max16(MFMA32(k1, qa, zero)));
    mb = fmaxf(max16(MFMA32(k0, qb, zero)), max16(MFMA32(k1, qb, zero)));
    ma = fmaxf(ma, __shfl_xor(ma, 32)) + M_OFF;
    mb = fmaxf(mb, __shfl_xor(mb, 32)) + M_OFF;
  }
  f32x16 oa, ob;
  for (int attempt = 0; attempt < 2; ++attempt) {
    f32x16 ca, cb;
    for (int i = 0; i < 16; ++i) { ca[i] = -ma; cb[i] = -mb; oa[i] = 0.f; ob[i] = 0.f; }
    attn_pass(KL, vaddr, vstride, qa, qb, ca, cb, oa, ob);
    bool ok = (oa[8] < 0x1p100f) && (ob[8] < 0x1p100f);
    #pragma unroll
    for (int i = 0; i < 8; ++i) ok = ok && (fabsf(oa[i]) < INFINITY) && (fabsf(ob[i]) < INFINITY);
    if (__all(ok) || attempt == 1) break;
    ma = -INFINITY; mb = -INFINITY;
    for (int t = 0; t < 32; ++t) {
      bf16x8 kf = lds_rd128(KL + t * 1024);
      ma = fmaxf(ma, max16(MFMA32(kf, qa, zero)));
      mb = fmaxf(mb, max16(MFMA32(kf, qb, zero)));
    }
    ma = fmaxf(ma, __shfl_xor(ma, 32));
    mb = fmaxf(mb, __shfl_xor(mb, 32));
  }
  __syncthreads();
  LAS float* ex = (LAS float*)smem;
  {
    const f32x16& give = kh ? oa : ob;
    float gm = kh ? ma : mb;
    #pragma unroll
    for (int i = 0; i < 9; ++i) ex[(wid * 10 + i) * 64 + lane] = give[i];
    ex[(wid * 10 + 9) * 64 + lane] = gm;
  }
  __syncthreads();
  {
    const f32x16& mine = kh ? ob : oa;
    float mm = kh ? mb : ma;
    int pw = wid ^ 4;
    float om = ex[(pw * 10 + 9) * 64 + lane];
    float mc = fmaxf(mm, om);
    float f0 = __builtin_amdgcn_exp2f(mm - mc), f1 = __builtin_amdgcn_exp2f(om - mc);
    float l = mine[8] * f0 + ex[(pw * 10 + 8) * 64 + lane] * f1;
    float inv = 1.0f / l;
    f0 *= inv; f1 *= inv;
    float o[8];
    #pragma unroll
    for (int i = 0; i < 8; ++i) o[i] = mine[i] * f0 + ex[(pw * 10 + i) * 64 + lane] * f1;
    u32x4 ov = {pk_bf16(o[0], o[1]), pk_bf16(o[2], o[3]), pk_bf16(o[4], o[5]), pk_bf16(o[6], o[7])};
    *(u32x4*)(ws + WS_OB + ((size_t)(pt * 32 + head0 + kh) * 64 + lane) * 16) = ov;
  }
}

__global__ __launch_bounds__(512, 2) void k_out(const uint16_t* __restrict__ awo, const uint16_t* __restrict__ bwo,
                                               const char* __restrict__ ws, uint16_t* __restrict__ out) {
  __shared__ __attribute__((aligned(16))) char smem_[131072 + 2 * 9216];
  LAS char* smem = (LAS char*)smem_;
  const int tid = threadIdx.x, lane = tid & 63, wid = __builtin_amdgcn_readfirstlane(tid >> 6);
  const int b = blockIdx.x, xcd = b & 7, idx = b >> 3, pb = xcd * 4 + (idx >> 3), ns = idx & 7;
  const int r = lane & 31, h = lane >> 5;
  const int ch = wid & 1, ph = (wid >> 1) & 1, ks = wid >> 2;
  stage_w64(awo, 512, ns * 64, 0, 512, smem, wid, lane, 8);
  stage_w64(bwo, 512, ns * 64, 0, 512, smem + 65536, wid, lane, 8);
  const int pt = pb * 2 + ph;
  bf16x8 of[2][16];
  #pragma unroll
  for (int br = 0; br < 2; ++br)
    #pragma unroll
    for (int s = 0; s < 16; ++s)
      of[br][s] = *(const bf16x8*)(ws + (br ? WS_OB : WS_OA) + ((size_t)(pt * 32 + ks * 16 + s) * 64 + lane) * 16);
  WAIT_VM0();
  __syncthreads();
  f32x16 acc[2];
  const int wtb = wtr_base(lane, ch * 32) + (4 * h) * 128;
  #pragma unroll
  for (int br = 0; br < 2; ++br) {
    for (int i = 0; i < 16; ++i) acc[br][i] = 0.f;
    LAS const char* wb = smem + br * 65536 + wtb + ks * 16 * 2048;
    #pragma unroll
    for (int s = 0; s < 16; ++s) {
      bf16x8 wf = cat8(lds_tr(wb + s * 2048), lds_tr(wb + s * 2048 + 1024));
      acc[br] = MFMA32(wf, of[br][s], acc[br]);
    }
  }
  LAS float* red;
  __syncthreads();
  red = (LAS float*)smem;
  LAS char* ot = smem + 131072;
  if (ks == 1) {
    #pragma unroll
    for (int br = 0; br < 2; ++br)
      #pragma unroll
      for (int i = 0; i < 16; ++i) red[((br * 4 + (wid & 3)) * 16 + i) * 64 + lane] = acc[br][i];
  }
  __syncthreads();
  if (ks == 0) {
    #pragma unroll
    for (int br = 0; br < 2; ++br) {
      #pragma unroll
      for (int g = 0; g < 4; ++g) {
        float v[4];
        #pragma unroll
        for (int j = 0; j < 4; ++j) v[j] = acc[br][4 * g + j] + red[((br * 4 + (wid & 3)) * 16 + 4 * g + j) * 64 + lane];
        u32x2 o = {pk_bf16(v[0], v[1]), pk_bf16(v[2], v[3])};
        *(LAS u32x2*)(ot + br * 9216 + (ph * 32 + r) * 144 + (ch * 32 + 8 * g + 4 * h) * 2) = o;
      }
    }
  }
  __syncthreads();
  {
    int pos = tid >> 3, c = tid & 7;
    #pragma unroll
    for (int br = 0; br < 2; ++br) {
      u32x4 v = *(LAS const u32x4*)(ot + br * 9216 + pos * 144 + c * 16);
      *(u32x4*)(out + (size_t)(pb * 64 + pos) * 1024 + br * 512 + ns * 64 + c * 8) = v;
    }
  }
}

extern "C" void kernel_launch(void* const* d_in, const int* in_sizes, int n_in, void* d_out, int out_size, void* d_ws, size_t ws_size,
                              hipStream_t stream) {
  const uint16_t* ax = (const uint16_t*)d_in[0];
  const uint16_t* awqkv = (const uint16_t*)d_in[1];
  const uint16_t* awo = (const uint16_t*)d_in[2];
  const uint16_t* bx = (const uint16_t*)d_in[3];
  const uint16_t* bwq = (const uint16_t*)d_in[4];
  const uint16_t* bwk = (const uint16_t*)d_in[5];
  const uint16_t* bwv = (const uint16_t*)d_in[6];
  const uint16_t* bwo = (const uint16_t*)d_in[7];
  char* ws = (char*)d_ws;
  k_proj<<<256, 512, 0, stream>>>(ax, awqkv, bx, bwq, bwk, bwv, ws);
  k_attn<<<256, 512, 0, stream>>>(ws);
  k_out<<<256, 512, 0, stream>>>(awo, bwo, ws, (uint16_t*)d_out);
}
```

```cpp
#include <hip/hip_runtime.h>
#include <hip/hip_bf16.h>
#include <stdint.h>
#include <type_traits>

typedef short bf16x8 __attribute__((ext_vector_type(8)));
typedef short s16x4 __attribute__((ext_vector_type(4)));
typedef short v4i16_t __attribute__((ext_vector_type(4)));
typedef float f32x16 __attribute__((ext_vector_type(16)));
typedef float f32x4 __attribute__((ext_vector_type(4)));
typedef float f32x2 __attribute__((ext_vector_type(2)));
typedef __bf16 bf16x2_t __attribute__((ext_vector_type(2)));
typedef unsigned u32x4 __attribute__((ext_vector_type(4)));
typedef unsigned u32x2 __attribute__((ext_vector_type(2)));
typedef unsigned long long u64;
typedef __attribute__((address_space(1))) u64 gu64;
typedef __attribute__((address_space(4))) const u64 cu64;
#define LAS __attribute__((address_space(3)))
#define DI __device__ __forceinline__
#define MFMA32(a, b, c) __builtin_amdgcn_mfma_f32_32x32x16_bf16((a), (b), (c), 0, 0, 0)
#define EXP2(x) __builtin_amdgcn_exp2f(x)

constexpr float QSCALE = 0.25f * 1.4426950408889634f;
constexpr float M_OFF = 40.f;

constexpr unsigned WS_EPOCH = 0;
constexpr unsigned WS_FKV = 4096;
constexpr unsigned WS_FV = 90112;
constexpr unsigned WS_FOA = 16384;
constexpr unsigned WS_FOB = 16384 + 32768;
constexpr unsigned WS_KV = 1u << 17;
constexpr unsigned WS_OA = 1u << 20;
constexpr unsigned WS_OB = 3u << 20;

constexpr int SLOT = 40960;
constexpr int IMG_Q = 131072, IMG_K = IMG_Q + 8192, IMG_V = IMG_K + 10240, VC_OFF = IMG_V + 10240;
constexpr int CTL_OFF = VC_OFF + 2048;
constexpr int LDS_BYTES = 163840;
constexpr int CODE_BLKS = 6;
#define PF 1
#define ROT 0
constexpr int NPF = PF ? 1 : 0;

#define ST(i)

DI unsigned pk_bf16(float lo, float hi) {
  f32x2 v = {lo, hi};
  bf16x2_t r = __builtin_convertvector(v, bf16x2_t);
  return __builtin_bit_cast(unsigned, r);
}
DI float dot2bf(unsigned a, unsigned b, float c) { return __builtin_amdgcn_fdot2_f32_bf16(__builtin_bit_cast(bf16x2_t, a), __builtin_bit_cast(bf16x2_t, b), c, false); }
DI bf16x8 lds_rd128(LAS const char* p) { return *(LAS const bf16x8*)p; }
DI s16x4 lds_tr(LAS const char* p) { return __builtin_bit_cast(s16x4, __builtin_amdgcn_ds_read_tr16_b64_v4i16((LAS v4i16_t*)p)); }
DI bf16x8 cat8(s16x4 a, s16x4 b) { return __builtin_shufflevector(a, b, 0, 1, 2, 3, 4, 5, 6, 7); }
DI float xsum32(float x) { auto t = __builtin_amdgcn_permlane32_swap(__float_as_uint(x), __float_as_uint(x), false, false); return __uint_as_float(t[0]) + __uint_as_float(t[1]); }
DI float xmax32(float x) { auto t = __builtin_amdgcn_permlane32_swap(__float_as_uint(x), __float_as_uint(x), false, false); return fmaxf(__uint_as_float(t[0]), __uint_as_float(t[1])); }

DI void dma_s(unsigned voff, const char* sbase, unsigned lds) {
  unsigned keep;
  asm volatile("s_mov_b32 %0, m0\n\ts_mov_b32 m0, %3\n\ts_nop 2\n\tglobal_load_lds_dwordx4 %1, %2\n\ts_mov_b32 m0, %0"
               : "=&s"(keep) : "v"(voff), "s"(sbase), "s"(lds) : "memory");
}
DI void dma_v(const char* vaddr, unsigned lds) {
  unsigned keep;
  asm volatile("s_mov_b32 %0, m0\n\ts_mov_b32 m0, %2\n\ts_nop 0\n\tglobal_load_lds_dwordx4 %1, off\n\ts_mov_b32 m0, %0"
               : "=&s"(keep) : "v"(vaddr), "s"(lds) : "memory");
}
DI void dma_sc1(unsigned voff, const char* sbase, unsigned lds) {
  unsigned keep;
  asm volatile("s_mov_b32 %0, m0\n\ts_mov_b32 m0, %3\n\ts_nop 2\n\tglobal_load_lds_dwordx4 %1, %2 sc1\n\ts_mov_b32 m0, %0"
               : "=&s"(keep) : "v"(voff), "s"(sbase), "s"(lds) : "memory");
}
DI void flag_ld(u64& d, unsigned voff, const char* sbase) { asm volatile("s_nop 4\n\tglobal_load_dwordx2 %0, %1, %2 sc1" : "=v"(d) : "v"(voff), "s"(sbase) : "memory"); }
DI void pf_s(unsigned& d, unsigned voff, const char* sbase) { asm volatile("s_nop 4\n\tglobal_load_dword %0, %1, %2" : "+v"(d) : "v"(voff), "s"(sbase) : "memory"); }
DI void pf_v(unsigned& d, const char* vaddr) { asm volatile("global_load_dword %0, %1, off sc1" : "+v"(d) : "v"(vaddr) : "memory"); }
DI void ld_sc1(u32x4& d, unsigned voff, const char* sbase) { asm volatile("s_nop 4\n\tglobal_load_dwordx4 %0, %1, %2 sc1" : "=v"(d) : "v"(voff), "s"(sbase) : "memory"); }
DI void st_sc1(unsigned voff, const u32x4& d, char* sbase) { asm volatile("s_nop 4\n\tglobal_store_dwordx4 %0, %1, %2 sc1\n\ts_nop 1" :: "v"(voff), "v"(d), "s"(sbase) : "memory"); }
#define WAITVM(n) asm volatile("s_waitcnt vmcnt(" #n ")" ::: "memory")
#define BAR() do { asm volatile("" ::: "memory"); __builtin_amdgcn_s_waitcnt(0xC07F); asm volatile("s_barrier" ::: "memory"); } while (0)
#define SB() __builtin_amdgcn_sched_barrier(0)
DI void waitvm_dyn(int n) {
  switch (n) {
    case 1: WAITVM(1); break; case 2: WAITVM(2); break; case 3: WAITVM(3); break; case 4: WAITVM(4); break; case 5: WAITVM(5); break;
    case 6: WAITVM(6); break; case 7: WAITVM(7); break; case 8: WAITVM(8); break; case 9: WAITVM(9); break; case 10: WAITVM(10); break;
    case 12: WAITVM(12); break; case 13: WAITVM(13); break;
    default: WAITVM(0); break;
  }
}
constexpr unsigned SPIN_CAP = 1u << 22;
DI void poll_flags(char* base, int n, u64 tag, int lane) {
  gu64* p = (gu64*)(base + (lane < n ? lane : 0) * 128);
  for (unsigned s = 0; s < SPIN_CAP; ++s) {
    u64 v = __hip_atomic_load(p, __ATOMIC_RELAXED, __HIP_MEMORY_SCOPE_AGENT);
    if (__all(v == tag)) break;
    __builtin_amdgcn_s_sleep(1);
  }
}
DI void poll_flags2(char* base0, char* base1, u64 tag, int lane) {
  gu64* p0 = (gu64*)(base0 + lane * 128);
  gu64* p1 = (gu64*)(base1 + lane * 128);
  for (unsigned s = 0; s < SPIN_CAP; ++s) {
    u64 v0 = __hip_atomic_load(p0, __ATOMIC_RELAXED, __HIP_MEMORY_SCOPE_AGENT);
    u64 v1 = __hip_atomic_load(p1, __ATOMIC_RELAXED, __HIP_MEMORY_SCOPE_AGENT);
    if (__all(v0 == tag && v1 == tag)) break;
    __builtin_amdgcn_s_sleep(1);
  }
}
DI bool test_flags(char* base, int n, u64 tag, int lane) {
  gu64* p = (gu64*)(base + (lane < n ? lane : 0) * 128);
  u64 v = __hip_atomic_load(p, __ATOMIC_RELAXED, __HIP_MEMORY_SCOPE_AGENT);
  return __all(v == tag);
}
DI void set_flag(char* p, u64 tag) { __hip_atomic_store((gu64*)p, tag, __ATOMIC_RELAXED, __HIP_MEMORY_SCOPE_AGENT); }

DI int wtr_base(int lane, int n0) {
  int g = lane >> 4, cb = g & 1, i = lane & 15, q = i >> 2, p = i & 3;
  int cidx = (n0 >> 3) + 2 * cb + (p >> 1);
  return q * 128 + ((cidx ^ ((q >> 1) << 2)) << 4) + (p & 1) * 8;
}
DI float max16(const f32x16& s) {
  float m0 = fmaxf(fmaxf(s[0], s[1]), s[2]), m1 = fmaxf(fmaxf(s[3], s[4]), s[5]);
  float m2 = fmaxf(fmaxf(s[6], s[7]), s[8]), m3 = fmaxf(fmaxf(s[9], s[10]), s[11]);
  float m4 = fmaxf(fmaxf(s[12], s[13]), s[14]);
  return fmaxf(fmaxf(fmaxf(m0, m1), fmaxf(m2, m3)), fmaxf(m4, s[15]));
}

#define E_(r) "v_exp_f32 v" #r ", v" #r "\n\t"
#define C_(d, a, b) "v_cvt_pk_bf16_f32 v" #d ", v" #a ", v" #b "\n\t"
#define MF_(d, a, b, c) "v_mfma_f32_32x32x16_bf16 " d ", " a ", " b ", " c "\n\t"
#define R_OA "v[0:15]"
#define R_OB "v[16:31]"
#define R_CA "v[32:47]"
#define R_CB "v[48:63]"
#define R_S0 "v[64:79]"
#define R_S1 "v[80:95]"
#define R_QA "v[96:99]"
#define R_QB "v[100:103]"
#define R_K "v[104:107]"
#define R_V0 "v[108:111]"
#define R_V1 "v[112:115]"
#define R_P0 "v[116:119]"
#define R_P1 "v[120:123]"
#define HALF_A(OV1, OK)                                                                                        \
  E_(64) E_(65) MF_(R_OB, R_V1, R_P1, R_OB) "ds_read_b128 " R_V1 ", v125 offset:" #OV1 "\n\t"                 \
  E_(66) E_(67) E_(68) E_(69) MF_(R_S1, R_K, R_QB, R_CB) "ds_read_b128 " R_K ", v124 offset:" #OK "\n\t"      \
  E_(70) E_(71) C_(116, 64, 65) C_(117, 66, 67) C_(118, 68, 69) C_(119, 70, 71) E_(72) E_(73)                  \
  "s_waitcnt lgkmcnt(2)\n\t" MF_(R_OA, R_V0, R_P0, R_OA)                                                       \
  E_(74) E_(75) E_(76) E_(77) E_(78) E_(79) C_(120, 72, 73) C_(121, 74, 75) C_(122, 76, 77) C_(123, 78, 79)
#define HALF_B(PRE, OV0)                                                                                       \
  E_(80) E_(81) "s_waitcnt lgkmcnt(1)\n\t" MF_(R_OA, R_V1, R_P1, R_OA)                                         \
  E_(82) E_(83) E_(84) E_(85) "s_waitcnt lgkmcnt(0)\n\t" MF_(R_S0, R_K, R_QA, R_CA)                            \
  E_(86) E_(87) C_(116, 80, 81) C_(117, 82, 83) C_(118, 84, 85) C_(119, 86, 87) E_(88) E_(89)                  \
  MF_(R_OB, R_V0, R_P0, R_OB) PRE "ds_read_b128 " R_V0 ", v125 offset:" #OV0 "\n\t"                            \
  E_(90) E_(91) E_(92) E_(93) E_(94) E_(95) C_(120, 88, 89) C_(121, 90, 91) C_(122, 92, 93) C_(123, 94, 95)

#define PRIOMODE 2
#define PRIO_(p) "s_setprio " #p "\n\t"
DI void attn_run(int npair, f32x16& oa, f32x16& ob, const f32x16& ca, const f32x16& cb, f32x16& s0, const bf16x8& qa, const bf16x8& qb,
                 bf16x8& kf, bf16x8& v0, bf16x8& v1, bf16x8& p1, unsigned& kaddr, unsigned& vaddr, unsigned vstride2, int young) {
  asm volatile(
      "s_nop 7\n\ts_nop 7\n"
      "1:\n\t"
      PRIO_(1)
      HALF_A(512, 1024)
      HALF_B("", 1024)
      PRIO_(0)
      HALF_A(1536, 2048)
      HALF_B("v_add_u32 v125, v125, v126\n\t", 0)
      "v_add_u32 v124, 0x800, v124\n\t"
      "s_sub_u32 %[n], %[n], 1\n\t"
      "s_cmp_lg_u32 %[n], 0\n\t"
      "s_cbranch_scc1 1b\n\t"
      "s_setprio 0\n\ts_waitcnt lgkmcnt(0)\n\ts_nop 7\n\ts_nop 7"
      : "+{v[0:15]}"(oa), "+{v[16:31]}"(ob), "+{v[64:79]}"(s0), "+{v[104:107]}"(kf), "+{v[108:111]}"(v0), "+{v[112:115]}"(v1),
        "+{v[120:123]}"(p1), "+{v124}"(kaddr), "+{v125}"(vaddr), [n] "+s"(npair)
      : [yw] "s"(young), "{v[32:47]}"(ca), "{v[48:63]}"(cb), "{v[96:99]}"(qa), "{v[100:103]}"(qb), "{v126}"(vstride2)
      : "v80", "v81", "v82", "v83", "v84", "v85", "v86", "v87", "v88", "v89", "v90", "v91", "v92", "v93", "v94", "v95",
        "v116", "v117", "v118", "v119", "scc", "memory");
}

DI void outproj_mfma(LAS const char* wpiece, const u32x4 (&of)[8], int lane, f32x16& acc0, f32x16& acc1) {
  const int h = lane >> 5;
  LAS const char* w0 = wpiece + wtr_base(lane, 0) + (4 * h) * 128;
  LAS const char* w1 = wpiece + wtr_base(lane, 32) + (4 * h) * 128;
  #pragma unroll
  for (int i = 0; i < 16; ++i) { acc0[i] = 0.f; acc1[i] = 0.f; }
  #pragma unroll
  for (int s = 0; s < 8; ++s) {
    bf16x8 wf0 = cat8(lds_tr(w0 + s * 2048), lds_tr(w0 + s * 2048 + 1024));
    bf16x8 wf1 = cat8(lds_tr(w1 + s * 2048), lds_tr(w1 + s * 2048 + 1024));
    bf16x8 o = __builtin_bit_cast(bf16x8, of[s]);
    acc0 = MFMA32(wf0, o, acc0);
    acc1 = MFMA32(wf1, o, acc1);
  }
}
DI void outproj_give(LAS char* area, int ph, int kq, int lane, const f32x16& acc0, const f32x16& acc1) {
  LAS char* p = area + (ph * 4 + kq) * 2048 + lane * 16;
  *(LAS f32x4*)(p + 0 * 32768) = f32x4{acc0[0], acc0[1], acc0[2], acc0[3]};
  *(LAS f32x4*)(p + 0 * 32768 + 1024) = f32x4{acc0[4], acc0[5], acc0[6], acc0[7]};
  *(LAS f32x4*)(p + 1 * 32768) = f32x4{acc0[8], acc0[9], acc0[10], acc0[11]};
  *(LAS f32x4*)(p + 1 * 32768 + 1024) = f32x4{acc0[12], acc0[13], acc0[14], acc0[15]};
  *(LAS f32x4*)(p + 2 * 32768) = f32x4{acc1[0], acc1[1], acc1[2], acc1[3]};
  *(LAS f32x4*)(p + 2 * 32768 + 1024) = f32x4{acc1[4], acc1[5], acc1[6], acc1[7]};
  *(LAS f32x4*)(p + 3 * 32768) = f32x4{acc1[8], acc1[9], acc1[10], acc1[11]};
  *(LAS f32x4*)(p + 3 * 32768 + 1024) = f32x4{acc1[12], acc1[13], acc1[14], acc1[15]};
}
DI void outproj_take(LAS const char* area, int ph, int kq, int lane, unsigned ooff, char* out) {
  const int c = lane >> 3;
  LAS const char* p = area + (c >> 1) * 32768 + ph * 4 * 2048 + (c & 1) * 1024 + (kq * 8 + (lane & 7)) * 16;
  f32x4 a = *(LAS const f32x4*)(p), b = *(LAS const f32x4*)(p + 512);
  #pragma unroll
  for (int s = 1; s < 4; ++s) { a += *(LAS const f32x4*)(p + s * 2048); b += *(LAS const f32x4*)(p + s * 2048 + 512); }
  const u32x4 o = {pk_bf16(a[0], a[1]), pk_bf16(a[2], a[3]), pk_bf16(b[0], b[1]), pk_bf16(b[2], b[3])};
  st_sc1(ooff, o, out);
}

__global__ __launch_bounds__(512, 2) void k_fused(const char* __restrict__ ax, const char* __restrict__ awqkv, const char* __restrict__ awo,
                                                 const char* __restrict__ bx, const char* __restrict__ bwq, const char* __restrict__ bwk,
                                                 const char* __restrict__ bwv, const char* __restrict__ bwo, char* __restrict__ ws,
                                                 uint16_t* __restrict__ out) {
  __shared__ __attribute__((aligned(16))) char smem_[LDS_BYTES];
  LAS char* smem = (LAS char*)smem_;
  const unsigned lds0 = (unsigned)(uintptr_t)smem;
  const int tid = threadIdx.x, lane = tid & 63, wid = __builtin_amdgcn_readfirstlane(tid >> 6);
  const int b = blockIdx.x, xcd = b & 7, idx = b >> 3, pb = xcd * 4 + (idx >> 3), hg = idx & 7;
  const int r = lane & 31, h = lane >> 5;
  const bool kvprod = hg < 2;
  ST(0);
  unsigned pfd = 0;
  {
    const char* pc; asm volatile("s_getpc_b64 %0" : "=s"(pc));
    pc = (const char*)((uintptr_t)pc & ~(uintptr_t)255);
    if (wid == 7 && idx < CODE_BLKS) pf_s(pfd, lane * 128, pc + idx * 8192 + 512);
  }

  asm volatile("" :: "s"(ax), "s"(awqkv), "s"(awo), "s"(bx), "s"(bwq), "s"(bwk), "s"(bwv), "s"(bwo), "s"(ws), "s"(out));
  const int rowB = 8 * wid + (lane >> 3);
  const unsigned offX = rowB * 1024 + (((lane & 7) ^ ((rowB >> 1) & 7)) << 4);
  const unsigned offW = rowB * 1024 + (((lane & 7) ^ (((lane >> 4) & 1) << 2)) << 4);
  const char* kvsrc = (((lane & 3) < 2) ? bwk : bwv) + (16 * wid + (lane >> 2)) * 32 + (lane & 1) * 16;
  auto stage_b = [&](int i, int m) {
    const unsigned sb = lds0 + i * SLOT + wid * 1024;
    const int ii = (i + hg) & 3;
    const char* xb = bx + (size_t)pb * 65536 + ii * 256;
    const char* wq = bwq + hg * 128 + ii * 131072;
    if (m == 0) dma_s(offX, xb, sb);
    if (m == 1) dma_s(offW, wq, sb + 16384);
    if (m == 2) dma_s(offX, xb + 128, sb + 8192);
    if (m == 3) dma_s(offW, wq + 65536, sb + 16384 + 8192);
    if (m == 4 && kvprod) dma_v(kvsrc + ii * 4096, sb + 32768);
  };
  #pragma unroll
  for (int i = 0; i < 2; ++i) {
    #pragma unroll
    for (int m = 0; m < 5; ++m) stage_b(i, m);
  }
  const u64 tag = *(cu64*)(ws + WS_EPOCH) + 1;
  const int nA = wid < 2 ? 5 : 4, nB = kvprod ? 5 : 4;
  const unsigned offWA = (lane >> 3) * 3072 + (((lane & 7) ^ (((lane >> 4) & 1) << 2)) << 4);
  auto offXA = [&](int m) -> unsigned {
    int row = 8 * m + (lane >> 3), g = pb * 64 - 16 + row;
    g = g < 0 ? 0 : g;
    return (unsigned)g * 1024 + (((lane & 7) ^ ((row >> 1) & 7)) << 4);
  };
  const unsigned offXA0 = offXA(wid), offXA1 = offXA(8 + (wid & 1));
  auto stage_a = [&](int j, int m) {
    const unsigned sb = lds0 + (j & 3) * SLOT + wid * 1024;
    j = (j + hg) & 7;
    if (m < 3) dma_s(offWA, awqkv + (size_t)(64 * j + 8 * wid) * 3072 + m * 1024 + hg * 128, sb + 10240 + m * 8192);
    if (m == 3) dma_s(offXA0, ax + j * 128, sb);
    if (m == 4 && wid < 2) dma_s(offXA1, ax + j * 128, sb + 8192);
  };

  const int ch = wid & 1, ph = (wid >> 1) & 1, kh = wid >> 2;
  const int nS = kvprod ? (wid == 6 ? 1 : (wid == 7 ? 2 : 0)) : 0;
  bf16x8 qa, qb;
  {
    f32x16 acc;
    #pragma unroll
    for (int i = 0; i < 16; ++i) acc[i] = 0.f;
    const int xrow = ph * 32 + r, xsw = (xrow >> 1) & 7;
    const int wtb = 16384 + wtr_base(lane, ch * 32) + (8 * h) * 128;
    const int krow = (hg & 1) * 32 + r;
    const int g4 = lane >> 4, i16 = lane & 15;
    const int kvb = 32768 + (8 * (g4 >> 1) + (i16 >> 2)) * 64 + (g4 & 1) * 32 + (i16 & 3) * 8;
    auto b_phase = [&](auto KV) {
      constexpr int KVM = decltype(KV)::value;
      bf16x8 fw[2][4], fx[2][4];
      #pragma unroll
      for (int t = 0; t <= 8; ++t) {
        const int i = t >> 1, u = t & 1;
        if (t < 8) {
          if (u == 0) {
            waitvm_dyn(i == 0 ? nB : (i == 1 ? 2 * nB : (i == 2 ? nB + nA : 2 * nA)));
            BAR();
          }
        }
        LAS const char* sb = smem + (i & 3) * SLOT + u * 8192;
        #pragma unroll
        for (int s = 0; s < 4; ++s) {
          SB();
          if (t > 0 && KVM == 0) acc = MFMA32(fw[u ^ 1][s], fx[u ^ 1][s], acc);
          if (t > 0 && KVM == 1) acc = MFMA32(fw[u ^ 1][s], fx[u ^ 1][s], acc);
          if (t > 0 && KVM == 2) acc = MFMA32(fx[u ^ 1][s], fw[u ^ 1][s], acc);
          SB();
          if (t < 8 && KVM == 0) {
            fw[u][s] = cat8(lds_tr(sb + wtb + s * 2048), lds_tr(sb + wtb + s * 2048 + 512));
            fx[u][s] = lds_rd128(sb + xrow * 128 + (((2 * s + h) ^ xsw) << 4));
          }
          if (t < 8 && (KVM == 1 || KVM == 2)) {
            LAS const char* wb = smem + i * SLOT + kvb + (u * 4 + s) * 1024;
            fw[u][s] = cat8(lds_tr(wb), lds_tr(wb + 256));
            fx[u][s] = lds_rd128(sb + krow * 128 + (((2 * s + h) ^ ((krow >> 1) & 7)) << 4));
          }
          SB();
          if (t < 8) {
            const int m = u * 2 + (s >> 1);
            if (i == 0) {
              if (t == 0) { stage_b(2, s); if (s == 3) stage_b(2, 4); }
              else { stage_b(3, s); if (s == 3) stage_b(3, 4); }
            } else if (s & 1) { stage_a(i - 1, m); if (m == 3) stage_a(i - 1, 4); }
          }
        }
        SB();
      }
    };
    if (wid < 4) b_phase(std::integral_constant<int, 0>{});
    else if (nS == 1) b_phase(std::integral_constant<int, 1>{});
    else if (nS == 2) b_phase(std::integral_constant<int, 2>{});
    else b_phase(std::integral_constant<int, 3>{});
    if (nS) {
      const int kt = pb * 2 + hg;
      const u32x4 t0 = {pk_bf16(acc[0], acc[1]), pk_bf16(acc[2], acc[3]), pk_bf16(acc[4], acc[5]), pk_bf16(acc[6], acc[7])};
      const u32x4 t1 = {pk_bf16(acc[8], acc[9]), pk_bf16(acc[10], acc[11]), pk_bf16(acc[12], acc[13]), pk_bf16(acc[14], acc[15])};
      if (nS == 1) st_sc1(kt * 1024 + lane * 16, t0, ws + WS_KV);
      else if (r >= 16) {
        st_sc1(65536 + kt * 1024 + (h * 16 + r - 16) * 16, t0, ws + WS_KV);
        st_sc1(65536 + kt * 1024 + ((2 + h) * 16 + r - 16) * 16, t1, ws + WS_KV);
      }
    }
    ST(1);
    u32x4 a = {pk_bf16(acc[0] * QSCALE, acc[1] * QSCALE), pk_bf16(acc[2] * QSCALE, acc[3] * QSCALE),
               pk_bf16(acc[4] * QSCALE, acc[5] * QSCALE), pk_bf16(acc[6] * QSCALE, acc[7] * QSCALE)};
    u32x4 bb = {pk_bf16(acc[8] * QSCALE, acc[9] * QSCALE), pk_bf16(acc[10] * QSCALE, acc[11] * QSCALE),
                pk_bf16(acc[12] * QSCALE, acc[13] * QSCALE), pk_bf16(acc[14] * QSCALE, acc[15] * QSCALE)};
    qa = __builtin_bit_cast(bf16x8, a);
    qb = __builtin_bit_cast(bf16x8, bb);
    if (wid < 4) {
      LAS char* p = smem + (wid >> 1) * SLOT + 34816 + (wid & 1) * 2048 + lane * 16;
      *(LAS u32x4*)p = a; *(LAS u32x4*)(p + 1024) = bb;
    }
  }
  ST(2);

  LAS volatile unsigned* ctl = (LAS volatile unsigned*)(smem + CTL_OFF);
  const unsigned kvoff = wid * 16384 + lane * 16;
  {
    const int grp = wid >> 1;
    f32x16 acc0, acc1;
    #pragma unroll
    for (int i = 0; i < 16; ++i) { acc0[i] = 0.f; acc1[i] = 0.f; }
    const int row0 = (grp == 0 ? 16 : (grp == 3 ? 48 : 0)) + r;
    const int xo0 = row0 * 128, sw0 = (row0 >> 1) & 7;
    const int wtb = 10240 + (grp == 3 ? 1 : grp) * 8192 + wtr_base(lane, ch * 32) + (8 * h) * 128;
    u64 fk = 0, fv = 0;
    auto a_phase = [&](auto HV) {
      constexpr bool HALO = decltype(HV)::value;
      bf16x8 f[2][12];
      #pragma unroll
      for (int j = 0; j <= 8; ++j) {
        if (j < 8) {
          if (j < 7) {
            waitvm_dyn((j < 6 ? 2 * nA : nA) + (j < 2 ? nS : ((j == 3 || j == 4) && nS ? 1 : 0)));
            if (j == 2 && nS && lane == 0) set_flag(ws + (nS == 1 ? WS_FKV : WS_FV) + (pb * 2 + hg) * 128, tag);
          } else WAITVM(0);
          BAR();
          if (j == 7 && HALO) { flag_ld(fk, lane * 128, ws + WS_FKV); flag_ld(fv, lane * 128, ws + WS_FV); }
          if (j == 0 && wid >= 4) {
            LAS const char* p = smem + ((wid >> 1) & 1) * SLOT + 34816 + (wid & 1) * 2048 + lane * 16;
            qa = lds_rd128(p); qb = lds_rd128(p + 1024);
          }
        }
        LAS const char* sb = smem + (j & 3) * SLOT;
        #pragma unroll
        for (int s = 0; s < 4; ++s) {
          const bf16x8 (&g)[12] = f[(j - 1) & 1];
          SB();
          if (j > 0) acc0 = MFMA32(g[3 * s], g[3 * s + 1], acc0);
          SB();
          if (j < 8) {
            f[j & 1][3 * s] = cat8(lds_tr(sb + wtb + s * 2048), lds_tr(sb + wtb + s * 2048 + 512));
            if (!HALO) f[j & 1][3 * s + 1] = lds_rd128(sb + xo0 + (((2 * s + h) ^ sw0) << 4));
          }
          SB();
          if (j > 0) {
            if (HALO) acc1 = MFMA32(g[3 * s + 2], g[3 * s + 1], acc1);
            else acc1 = MFMA32(g[3 * s], g[3 * s + 2], acc1);
          }
          SB();
          if (j < 8) {
            if (HALO) {
              f[j & 1][3 * s + 1] = lds_rd128(sb + xo0 + (((2 * s + h) ^ sw0) << 4));
              f[j & 1][3 * s + 2] = cat8(lds_tr(sb + wtb + 8192 + s * 2048), lds_tr(sb + wtb + 8192 + s * 2048 + 512));
            } else f[j & 1][3 * s + 2] = lds_rd128(sb + xo0 + 4096 + (((2 * s + h) ^ sw0) << 4));
          }
          SB();
          if (j + 3 < 8) { stage_a(j + 3, s); if (s == 3) stage_a(j + 3, 4); }
        }
        SB();
      }
    };
    if (grp < 3) a_phase(std::false_type{}); else a_phase(std::true_type{});
    ST(3);
    asm volatile("" :: "v"(pfd));
    BAR();
    #pragma unroll
    for (int a = 0; a < 2; ++a) {
      const int head = ch * 2 + a;
      u32x4 o0 = {pk_bf16(acc0[8 * a + 0], acc0[8 * a + 1]), pk_bf16(acc0[8 * a + 2], acc0[8 * a + 3]),
                  pk_bf16(acc0[8 * a + 4], acc0[8 * a + 5]), pk_bf16(acc0[8 * a + 6], acc0[8 * a + 7])};
      u32x4 o1 = {pk_bf16(acc1[8 * a + 0], acc1[8 * a + 1]), pk_bf16(acc1[8 * a + 2], acc1[8 * a + 3]),
                  pk_bf16(acc1[8 * a + 4], acc1[8 * a + 5]), pk_bf16(acc1[8 * a + 6], acc1[8 * a + 7])};
      if (grp == 0) {
        LAS char* p = smem + IMG_Q + ((head * 2 + h) * 64 + r) * 16;
        *(LAS u32x4*)p = o0; *(LAS u32x4*)(p + 512) = o1;
      } else if (grp < 3) {
        LAS char* p = smem + (grp == 1 ? IMG_K : IMG_V) + ((head * 2 + h) * 80 + r) * 16;
        *(LAS u32x4*)p = o0; *(LAS u32x4*)(p + 512) = o1;
      } else if (r >= 16) {
        const int o = ((head * 2 + h) * 80 + 48 + r) * 16;
        *(LAS u32x4*)(smem + IMG_K + o) = o0; *(LAS u32x4*)(smem + IMG_V + o) = o1;
      }
    }
    if (tid < 32) {
      const int blk = tid >> 3, sl = tid & 7;
      const unsigned v = ((sl & 3) == 0) ? 0x3f803f80u : 0u;
      *(LAS u32x4*)(smem + VC_OFF + blk * 512 + sl * 16) = u32x4{v, v, v, v};
    }
    if (grp == 3) {
      WAITVM(0);
      asm volatile("" : "+v"(fk), "+v"(fv));
      if (wid == 7 && !__all(fk == tag && fv == tag)) poll_flags2(ws + WS_FKV, ws + WS_FV, tag, lane);
    }
    BAR();
  }
  ST(4);
  {
    const int head = wid >> 1, pl = (wid & 1) * 32 + r, gpos = pb * 64 + pl;
    const u32x4 q = *(LAS const u32x4*)(smem + IMG_Q + ((head * 2 + h) * 64 + pl) * 16);
    LAS const char* kb = smem + IMG_K + ((head * 2 + h) * 80 + pl + 16) * 16;
    LAS const char* vb = smem + IMG_V + ((head * 2 + h) * 80 + pl + 16) * 16;
    float lg[8];
    #pragma unroll
    for (int w = 0; w < 8; ++w) {
      dma_sc1(kvoff, ws + WS_KV + (2 * w) * 1024, lds0 + wid * 16384 + (2 * w) * 1024);
      dma_sc1(kvoff, ws + WS_KV + (2 * w + 1) * 1024, lds0 + wid * 16384 + (2 * w + 1) * 1024);
      const u32x4 kk = *(LAS const u32x4*)(kb - 32 * w);
      float s = 0.f;
      #pragma unroll
      for (int i = 0; i < 4; ++i) s = dot2bf(q[i], kk[i], s);
      s = xsum32(s);
      lg[w] = (gpos - 2 * w >= 0) ? s * QSCALE : -INFINITY;
    }
    float m = lg[0];
    #pragma unroll
    for (int w = 1; w < 8; ++w) m = fmaxf(m, lg[w]);
    float l = 0.f;
    #pragma unroll
    for (int w = 0; w < 8; ++w) { lg[w] = EXP2(lg[w] - m); l += lg[w]; }
    const float inv = 1.0f / l;
    float o[8];
    #pragma unroll
    for (int j = 0; j < 8; ++j) o[j] = 0.f;
    #pragma unroll
    for (int w = 0; w < 8; w += 2) {
      const u32x4 va = *(LAS const u32x4*)(vb - 32 * w), vb2 = *(LAS const u32x4*)(vb - 32 * w - 32);
      const unsigned pp = pk_bf16(lg[w] * inv, lg[w + 1] * inv);
      #pragma unroll
      for (int i = 0; i < 4; ++i) {
        const unsigned lo = __builtin_amdgcn_perm(vb2[i], va[i], 0x05040100u), hi = __builtin_amdgcn_perm(vb2[i], va[i], 0x07060302u);
        o[2 * i] = dot2bf(lo, pp, o[2 * i]);
        o[2 * i + 1] = dot2bf(hi, pp, o[2 * i + 1]);
      }
    }
    const u32x4 ov = {pk_bf16(o[0], o[1]), pk_bf16(o[2], o[3]), pk_bf16(o[4], o[5]), pk_bf16(o[6], o[7])};
    st_sc1((((pb * 2 + (wid & 1)) * 32 + hg * 4 + head) * 64 + lane) * 16, ov, ws + WS_OA);
  }
  ST(5);

  const int ph2 = wid & 1, kq = wid >> 1;
  f32x16 oa, ob;
  float ma, mb;
  unsigned vaddr0, vstride2;
  if (r < 16) { vaddr0 = lds0 + 65536 + kh * 32768 + h * 256 + r * 16; vstride2 = 2048; }
  else { vaddr0 = lds0 + VC_OFF + ((r >= 20 && r < 28) ? 64 : 0) + ((r == 16 || r == 20) ? 0 : 16); vstride2 = 0; }
  WAITVM(1);
  BAR();
  for (int attempt = 0;; ++attempt) {
    ST(6);
    LAS const char* KL = smem + kh * 32768 + lane * 16;
    f32x16 zero;
    #pragma unroll
    for (int i = 0; i < 16; ++i) zero[i] = 0.f;
    bf16x8 kf = lds_rd128(KL);
    if (attempt == 0) {
      bf16x8 k1 = lds_rd128(KL + 1024);
      ma = fmaxf(max16(MFMA32(kf, qa, zero)), max16(MFMA32(k1, qa, zero)));
      mb = fmaxf(max16(MFMA32(kf, qb, zero)), max16(MFMA32(k1, qb, zero)));
      ma = xmax32(ma) + M_OFF; mb = xmax32(mb) + M_OFF;
    } else {
      ma = -INFINITY; mb = -INFINITY;
      #pragma unroll 1
      for (int t = 0; t < 32; ++t) {
        bf16x8 kt = lds_rd128(KL + t * 1024);
        ma = fmaxf(ma, max16(MFMA32(kt, qa, zero)));
        mb = fmaxf(mb, max16(MFMA32(kt, qb, zero)));
      }
      ma = xmax32(ma); mb = xmax32(mb);
    }
    f32x16 ca, cb;
    #pragma unroll
    for (int i = 0; i < 16; ++i) { ca[i] = -ma; cb[i] = -mb; oa[i] = 0.f; ob[i] = 0.f; }
    unsigned kaddr = lds0 + kh * 32768 + lane * 16, vaddr = vaddr0;
    bf16x8 v0 = *(LAS const bf16x8*)(uintptr_t)vaddr, v1, p1;
    #pragma unroll
    for (int i = 0; i < 8; ++i) { v1[i] = 0; p1[i] = 0; }
    f32x16 s0 = MFMA32(kf, qa, ca);
    ST(7);
    attn_run(8, oa, ob, ca, cb, s0, qa, qb, kf, v0, v1, p1, kaddr, vaddr, vstride2, kh);
    ST(8);
    WAITVM(0);
    BAR();
    if (attempt == 0 && wid == 7 && lane == 0) set_flag(ws + WS_FOA + (pb * 8 + hg) * 128, tag);
    {
      const unsigned offWO = (lane >> 3) * 1024 + (((lane & 7) ^ (((lane >> 4) & 1) << 2)) << 4);
      const char* src = awo + (size_t)(wid * 64) * 1024 + hg * 128;
      #pragma unroll
      for (int m = 0; m < 8; ++m) dma_s(offWO, src + m * 8192, lds0 + (wid >> 1) * 32768 + ((wid & 1) * 8 + m) * 1024);
    }
    if (wid == 7) {
      #pragma unroll
      for (int n = 0; n < 2; ++n) pf_s(pfd, lane * 1024, bwo + (size_t)((idx >> 3) * 2 + n) * 65536 + hg * 128);
    }
    attn_run(5, oa, ob, ca, cb, s0, qa, qb, kf, v0, v1, p1, kaddr, vaddr, vstride2, kh);
    u64 fo;
    flag_ld(fo, (lane & 7) * 128, ws + WS_FOA + pb * 8 * 128);
    attn_run(3, oa, ob, ca, cb, s0, qa, qb, kf, v0, v1, p1, kaddr, vaddr, vstride2, kh);
    ob = MFMA32(v1, p1, ob);
    ST(9);
    bool ok = (oa[8] < 0x1p100f) && (ob[8] < 0x1p100f);
    #pragma unroll
    for (int i = 0; i < 8; ++i) ok = ok && (fabsf(oa[i]) < INFINITY) && (fabsf(ob[i]) < INFINITY);
    const bool wok = __all(ok);
    {
      LAS float* ex = (LAS float*)(smem + IMG_Q) + wid * 640 + lane;
      #pragma unroll
      for (int i = 0; i < 9; ++i) ex[i * 64] = kh ? oa[i] : ob[i];
      ex[9 * 64] = kh ? ma : mb;
      if (lane == 0) ctl[8 + wid] = wok ? 1u : 0u;
    }
    WAITVM(0);
    asm volatile("" : "+v"(fo) : "v"(pfd));
    if (wid == 7 && !__all(fo == tag)) poll_flags(ws + WS_FOA + pb * 8 * 128, 8, tag, lane);
    BAR();
    const unsigned votes = ctl[8 + (lane & 7)];
    if (__all(votes != 0) || attempt == 1) break;
    BAR();
    #pragma unroll
    for (int i = 0; i < 16; ++i) dma_sc1(kvoff, ws + WS_KV + i * 1024, lds0 + wid * 16384 + i * 1024);
    WAITVM(0);
    BAR();
  }
  u32x4 of[8];
  const unsigned ooff = ((pb * 2 + ph2) * 32 + kq * 8) * 1024 + lane * 16;
  #pragma unroll
  for (int s = 0; s < 8; ++s) ld_sc1(of[s], ooff, ws + WS_OA + s * 1024);
  {
    LAS const float* ex = (LAS const float*)(smem + IMG_Q) + (wid ^ 4) * 640 + lane;
    const float mm = kh ? mb : ma, om = ex[9 * 64];
    const float mc = fmaxf(mm, om);
    float f0 = EXP2(mm - mc), f1 = EXP2(om - mc);
    const float l = (kh ? ob[8] : oa[8]) * f0 + ex[8 * 64] * f1;
    const float inv = 1.0f / l;
    f0 *= inv; f1 *= inv;
    float o[8];
    #pragma unroll
    for (int i = 0; i < 8; ++i) o[i] = (kh ? ob[i] : oa[i]) * f0 + ex[i * 64] * f1;
    const u32x4 ov = {pk_bf16(o[0], o[1]), pk_bf16(o[2], o[3]), pk_bf16(o[4], o[5]), pk_bf16(o[6], o[7])};
    st_sc1((((pb * 2 + ph) * 32 + hg * 4 + ch * 2 + kh) * 64 + lane) * 16, ov, ws + WS_OB);
  }
  {
    const unsigned offWO = (lane >> 3) * 1024 + (((lane & 7) ^ (((lane >> 4) & 1) << 2)) << 4);
    const char* src = bwo + (size_t)(wid * 64) * 1024 + hg * 128;
    #pragma unroll
    for (int m = 0; m < 8; ++m) dma_s(offWO, src + m * 8192, lds0 + (wid >> 1) * 32768 + 16384 + ((wid & 1) * 8 + m) * 1024);
  }
  ST(10);
  const unsigned oo = (pb * 64 + ph2 * 32 + kq * 8 + (lane & 7)) * 2048 + hg * 128 + (lane >> 3) * 16;
  f32x16 acc0, acc1;
  WAITVM(9);
  #pragma unroll
  for (int s = 0; s < 8; ++s) asm volatile("" : "+v"(of[s]));
  outproj_mfma(smem + kq * 32768, of, lane, acc0, acc1);
  ST(11);
  WAITVM(8);
  BAR();
  if (wid == 7 && lane == 0) set_flag(ws + WS_FOB + (pb * 8 + hg) * 128, tag);
  outproj_give(smem, ph2, kq, lane, acc0, acc1);
  BAR();
  outproj_take(smem, ph2, kq, lane, oo, (char*)out);
  ST(12);
  if (wid == 7) poll_flags(ws + WS_FOB + pb * 8 * 128, 8, tag, lane);
  if (wid == 6 && b == 0) {
    gu64* p = (gu64*)(ws + WS_FOA + lane * 128);
    for (unsigned s = 0; s < SPIN_CAP; ++s) {
      const u64 v0 = __hip_atomic_load(p, __ATOMIC_RELAXED, __HIP_MEMORY_SCOPE_AGENT), v1 = __hip_atomic_load(p + 1024, __ATOMIC_RELAXED, __HIP_MEMORY_SCOPE_AGENT);
      const u64 v2 = __hip_atomic_load(p + 2048, __ATOMIC_RELAXED, __HIP_MEMORY_SCOPE_AGENT), v3 = __hip_atomic_load(p + 3072, __ATOMIC_RELAXED, __HIP_MEMORY_SCOPE_AGENT);
      if (__all(v0 == tag && v1 == tag && v2 == tag && v3 == tag)) break;
      __builtin_amdgcn_s_sleep(1);
    }
    if (lane == 0) set_flag(ws + WS_EPOCH, tag);
  }
  WAITVM(1);
  BAR();
  ST(13);
  #pragma unroll
  for (int s = 0; s < 8; ++s) ld_sc1(of[s], ooff, ws + WS_OB + s * 1024);
  WAITVM(0);
  #pragma unroll
  for (int s = 0; s < 8; ++s) asm volatile("" : "+v"(of[s]));
  ST(14);
  outproj_mfma(smem + kq * 32768 + 16384, of, lane, acc0, acc1);
  outproj_give(smem, ph2, kq, lane, acc0, acc1);
  BAR();
  outproj_take(smem, ph2, kq, lane, oo + 1024, (char*)out);
  ST(15);
}

extern "C" void kernel_launch(void* const* d_in, const int* in_sizes, int n_in, void* d_out, int out_size, void* d_ws, size_t ws_size,
                              hipStream_t stream) {
  k_fused<<<256, 512, 0, stream>>>((const char*)d_in[0], (const char*)d_in[1], (const char*)d_in[2], (const char*)d_in[3],
                                   (const char*)d_in[4], (const char*)d_in[5], (const char*)d_in[6], (const char*)d_in[7],
                                   (char*)d_ws, (uint16_t*)d_out);
}
```
